```python
import torch

def _softmax(x, axis=-1):
    return torch.softmax(x, dim=axis)
def _gelu(x):
    return torch.nn.functional.gelu(x, approximate="tanh")
def _silu(x):
    return torch.nn.functional.silu(x)
def _sigmoid(x):
    return torch.sigmoid(x)
def _layernorm(x, axis=-1, eps=1e-5):
    m = x.mean(dim=axis, keepdim=True)
    v = x.var(dim=axis, keepdim=True, unbiased=False)
    return (x - m) / torch.sqrt(v + eps)
def _scatter_add(tgt, idx, src, axis=0):
    return tgt.clone().index_add_(axis, idx, src)
def _segment_sum(src, seg, n):
    out = torch.zeros((n,) + src.shape[1:], dtype=src.dtype)
    return out.index_add_(0, seg, src)
def _pad(x, pw):
    flat = tuple(v for pair in reversed(pw) for v in pair)
    return torch.nn.functional.pad(x, flat)

kernel_name = 'standalone__geglu_ffn__decode'

def setup_inputs():

    _w = (lambda a: (lambda t: t.to(torch.bfloat16) if t.is_floating_point() else t)(torch.as_tensor(a)))
    B, S, D, F = 1, 1, 1024, 3584
    return {
        "x": _w((lambda *s: torch.randn(*s, dtype=torch.bfloat16) * 0.05)(B, S, D)),
        "w_gate": _w((lambda *s: torch.randn(*s, dtype=torch.bfloat16) * 0.05)(D, F)),
        "w_up": _w((lambda *s: torch.randn(*s, dtype=torch.bfloat16) * 0.05)(D, F)),
        "w_down": _w((lambda *s: torch.randn(*s, dtype=torch.bfloat16) * 0.05)(F, D)),
    }

def reference(x, w_gate, w_up, w_down):
    return (_gelu(x @ w_gate) * (x @ w_up)) @ w_down
```

```cpp
#include <hip/hip_runtime.h>
#include <hip/hip_bf16.h>
#include <stdint.h>

typedef unsigned v4u __attribute__((ext_vector_type(4)));
typedef float v4f __attribute__((ext_vector_type(4)));
constexpr int D = 1024, F = 3584;
constexpr int NWG = 224;

__device__ __forceinline__ float bflo(unsigned u) { return __builtin_bit_cast(float, u << 16); }
__device__ __forceinline__ float bfhi(unsigned u) { return __builtin_bit_cast(float, u & 0xffff0000u); }
__device__ __forceinline__ float rbf(float f) { return __bfloat162float(__float2bfloat16(f)); }

__global__ void __launch_bounds__(256) k1(const unsigned short* __restrict__ x, const unsigned short* __restrict__ wg,
                                          const unsigned short* __restrict__ wu, const unsigned short* __restrict__ wd,
                                          float* __restrict__ part) {
  __shared__ float red[4][32];
  __shared__ float hs[16];
  __shared__ float o2[1024];
  const int b = blockIdx.x, t = threadIdx.x;
  const int slab = (b & 7) * 28 + (b >> 3);
  const int half = t & 1, r0 = t >> 1;
  v4u g[8], u[8], dn[8];
  float xv[8];
#pragma unroll
  for (int i = 0; i < 8; ++i) g[i] = *(const v4u*)(wg + (size_t)(r0 + 128 * i) * F + slab * 16 + half * 8);
#pragma unroll
  for (int i = 0; i < 8; ++i) u[i] = *(const v4u*)(wu + (size_t)(r0 + 128 * i) * F + slab * 16 + half * 8);
  const int dc = (t & 127) * 8, dr = (t >> 7) * 8;
#pragma unroll
  for (int i = 0; i < 8; ++i) dn[i] = *(const v4u*)(wd + (size_t)(slab * 16 + dr + i) * D + dc);
#pragma unroll
  for (int i = 0; i < 8; ++i) xv[i] = bflo(x[r0 + 128 * i]);
  float ag[8], au[8];
#pragma unroll
  for (int j = 0; j < 8; ++j) { ag[j] = 0.f; au[j] = 0.f; }
#pragma unroll
  for (int i = 0; i < 8; ++i) {
#pragma unroll
    for (int j = 0; j < 4; ++j) {
      ag[2 * j] += xv[i] * bflo(g[i][j]); ag[2 * j + 1] += xv[i] * bfhi(g[i][j]);
      au[2 * j] += xv[i] * bflo(u[i][j]); au[2 * j + 1] += xv[i] * bfhi(u[i][j]);
    }
  }
#pragma unroll
  for (int j = 0; j < 8; ++j) {
#pragma unroll
    for (int s = 2; s < 64; s <<= 1) { ag[j] += __shfl_xor(ag[j], s); au[j] += __shfl_xor(au[j], s); }
  }
  const int w = t >> 6, l = t & 63;
  if (l < 2) {
#pragma unroll
    for (int j = 0; j < 8; ++j) { red[w][l * 8 + j] = ag[j]; red[w][16 + l * 8 + j] = au[j]; }
  }
  __syncthreads();
  if (t < 16) {
    float gg = red[0][t] + red[1][t] + red[2][t] + red[3][t];
    float uu = red[0][16 + t] + red[1][16 + t] + red[2][16 + t] + red[3][16 + t];
    gg = rbf(gg); uu = rbf(uu);
    float inner = 0.7978845608028654f * (gg + 0.044715f * gg * gg * gg);
    float a = rbf(0.5f * gg * (1.f + tanhf(inner)));
    hs[t] = rbf(a * uu);
  }
  __syncthreads();
  float o[8];
#pragma unroll
  for (int j = 0; j < 8; ++j) o[j] = 0.f;
#pragma unroll
  for (int i = 0; i < 8; ++i) {
    float h = hs[dr + i];
#pragma unroll
    for (int j = 0; j < 4; ++j) { o[2 * j] += h * bflo(dn[i][j]); o[2 * j + 1] += h * bfhi(dn[i][j]); }
  }
  if (t >= 128) {
#pragma unroll
    for (int j = 0; j < 8; ++j) o2[dc + j] = o[j];
  }
  __syncthreads();
  if (t < 128) {
    v4f a, c;
#pragma unroll
    for (int j = 0; j < 4; ++j) { a[j] = o[j] + o2[dc + j]; c[j] = o[4 + j] + o2[dc + 4 + j]; }
    *(v4f*)(part + (size_t)b * D + dc) = a;
    *(v4f*)(part + (size_t)b * D + dc + 4) = c;
  }
}

__global__ void __launch_bounds__(256) k2(const float* __restrict__ part, __hip_bfloat16* __restrict__ out) {
  __shared__ float red[32][33];
  const int b = blockIdx.x, t = threadIdx.x;
  const int c4 = t & 7, p0 = t >> 3;
  v4f v[7];
#pragma unroll
  for (int i = 0; i < 7; ++i) v[i] = *(const v4f*)(part + (size_t)(p0 + 32 * i) * D + b * 32 + c4 * 4);
  v4f s = v[0];
#pragma unroll
  for (int i = 1; i < 7; ++i) s += v[i];
#pragma unroll
  for (int j = 0; j < 4; ++j) red[p0][c4 * 4 + j] = s[j];
  __syncthreads();
  if (t < 32) {
    float a = 0.f;
#pragma unroll
    for (int i = 0; i < 32; ++i) a += red[i][t];
    out[b * 32 + t] = __float2bfloat16(a);
  }
}

extern "C" void kernel_launch(void* const* d_in, const int* in_sizes, int n_in, void* d_out, int out_size, void* d_ws,
                              size_t ws_size, hipStream_t stream) {
  k1<<<NWG, 256, 0, stream>>>((const unsigned short*)d_in[0], (const unsigned short*)d_in[1], (const unsigned short*)d_in[2],
                              (const unsigned short*)d_in[3], (float*)d_ws);
  k2<<<32, 256, 0, stream>>>((const float*)d_ws, (__hip_bfloat16*)d_out);
}
```

```cpp
#include <hip/hip_runtime.h>
#include <hip/hip_bf16.h>
#include <stdint.h>

typedef unsigned v4u __attribute__((ext_vector_type(4)));
typedef unsigned v2u __attribute__((ext_vector_type(2)));
typedef float v4f __attribute__((ext_vector_type(4)));
typedef short bf16x8 __attribute__((ext_vector_type(8)));
#define GAS __attribute__((address_space(1)))
typedef GAS unsigned long long gu64;

constexpr int D = 1024, F = 3584;
constexpr int NWG = 224;
#define SH 2
#define STAMPS 0
#define STAMP_OFF (128u << 20)
#define NSTAMP 8
constexpr unsigned EPOCH_OFF = 0;
constexpr unsigned MID_OFF = 4096;
constexpr unsigned MID_BYTES = 2 * 56 * 8 * 32 * 16;
constexpr unsigned FIN_OFF = MID_OFF + MID_BYTES;
constexpr unsigned FIN_BYTES = 16 * 56 * 32 * 16;
constexpr unsigned WS_BYTES = FIN_OFF + FIN_BYTES;
constexpr int AUX_NT = 2, AUX_SC1 = 16;
constexpr unsigned SPIN_CAP = 1u << 20;

__device__ __forceinline__ float rbf(float f) {
  return __bfloat162float(__float2bfloat16(f));
}
__device__ __forceinline__ v4f mfma(v4u a, v4u b, v4f c) {
  return __builtin_amdgcn_mfma_f32_16x16x32_bf16(__builtin_bit_cast(bf16x8, a), __builtin_bit_cast(bf16x8, b), c, 0, 0, 0);
}
__device__ __forceinline__ float fadd_ror8(float v) {
  return v + __int_as_float(__builtin_amdgcn_update_dpp(0, __float_as_int(v), 0x128, 0xf, 0xf, true));
}
__device__ __forceinline__ float ror8_hi(float v) {
  return __int_as_float(__builtin_amdgcn_update_dpp(__float_as_int(v), __float_as_int(v), 0x128, 0xc, 0xf, false));
}
__device__ __forceinline__ float swap32_add(float a, float b) {
  auto r = __builtin_amdgcn_permlane32_swap(__float_as_uint(a), __float_as_uint(b), false, false);
  return __uint_as_float(r[0]) + __uint_as_float(r[1]);
}
__device__ __forceinline__ float swap16_add(float a, float b) {
  auto r = __builtin_amdgcn_permlane16_swap(__float_as_uint(a), __float_as_uint(b), false, false);
  return __uint_as_float(r[0]) + __uint_as_float(r[1]);
}
#define STAMP(k)

__global__ void __launch_bounds__(256, 1)
geglu_fused(const unsigned short* __restrict__ x, const unsigned short* __restrict__ wg, const unsigned short* __restrict__ wu,
            const unsigned short* __restrict__ wd, unsigned* __restrict__ out, unsigned char* ws) {
  STAMP(0);
  __shared__ __attribute__((aligned(16))) unsigned short hs[64];
  const int b = blockIdx.x, t = threadIdx.x, l = t & 63;
  const int w = __builtin_amdgcn_readfirstlane(t >> 6);
  const int grp = b >> 3, m = b & 7;
  const int n = l & 15, g = l >> 4;
  const int q = ((l >> 3) & 1) * 4 + g;
  const int c8 = l & 7;

  const __amdgpu_buffer_rsrc_t rws = __builtin_amdgcn_make_buffer_rsrc((void*)ws, 0, WS_BYTES, 0x00020000);
  const v2u ep = __builtin_amdgcn_raw_buffer_load_b64(rws, EPOCH_OFF, 0, AUX_SC1);
  const __amdgpu_buffer_rsrc_t rx = __builtin_amdgcn_make_buffer_rsrc((void*)x, 0, D * 2, 0x00020000);
  const v4u x0 = __builtin_amdgcn_raw_buffer_load_b128(rx, q * 32, m * 256, 0);
  const v4u x1 = __builtin_amdgcn_raw_buffer_load_b128(rx, q * 32 + 16, m * 256, 0);
  const int mat = w >> 1;
  const int piece = 2 * (mat ? (grp + SH) % 28 : grp) + (w & 1);
  const unsigned short* w1 = (mat ? wu : wg) + (size_t)(128 * m) * F + piece * 64;
  const __amdgpu_buffer_rsrc_t r1 = __builtin_amdgcn_make_buffer_rsrc((void*)w1, 0, 128 * F * 2, 0x00020000);
  v4u a[16];
  const int vo1 = q * 16 * F * 2 + c8 * 16;
#pragma unroll
  for (int s = 0; s < 16; ++s) a[s] = __builtin_amdgcn_raw_buffer_load_b128(r1, vo1, s * F * 2, AUX_NT);
  const int rh = m >> 2, cq = m & 3;
  const unsigned short* w2 = wd + (size_t)(128 * grp + 64 * rh) * D + 256 * cq + 64 * w;
  const __amdgpu_buffer_rsrc_t r2 = __builtin_amdgcn_make_buffer_rsrc((void*)w2, 0, 64 * D * 2, 0x00020000);
  v4u dn[8];
  const int vo2 = q * 8 * D * 2 + c8 * 16;
#pragma unroll
  for (int s = 0; s < 8; ++s) dn[s] = __builtin_amdgcn_raw_buffer_load_b128(r2, vo2 + (s & 1) * D * 2, (s >> 1) * D * 4, AUX_NT);
  __builtin_amdgcn_sched_barrier(0);
  STAMP(1);
  unsigned sel[2][4];
#pragma unroll
  for (int hb = 0; hb < 2; ++hb)
#pragma unroll
    for (int r = 0; r < 4; ++r) {
      const unsigned src = hb ? 0x0302u : 0x0100u;
      const unsigned on = (c8 & 1) ? ((src << 16) | 0x0c0cu) : (0x0c0c0000u | src);
      sel[hb][r] = ((c8 >> 1) == r) ? on : 0x0c0c0c0cu;
    }
  const unsigned long long e64 = (((unsigned long long)ep.y << 32) | ep.x) + 1ull;
  const unsigned tag0 = __builtin_amdgcn_readfirstlane((unsigned)e64), tag1 = __builtin_amdgcn_readfirstlane((unsigned)(e64 >> 32));
  v4f acc0 = {0.f, 0.f, 0.f, 0.f};
#pragma unroll
  for (int s = 0; s < 16; ++s) {
    const unsigned xs = (s < 8) ? x0[(s & 7) >> 1] : x1[(s & 7) >> 1];
    v4u fa;
#pragma unroll
    for (int r = 0; r < 4; ++r) fa[r] = __builtin_amdgcn_perm(0u, xs, sel[s & 1][r]);
    acc0 = mfma(fa, a[s], acc0);
  }
  STAMP(2);
  {
    float v0 = ror8_hi(acc0[0]), v1 = ror8_hi(acc0[1]), v2 = ror8_hi(acc0[2]), v3 = ror8_hi(acc0[3]);
    const float s0 = swap32_add(v0, v2), s1 = swap32_add(v1, v3);
    if (n < 8) {
      v4u gr = {__float_as_uint(s0), __float_as_uint(s1), tag0, tag1};
      const int pair = 4 * n + 2 * (g & 1) + (g >> 1);
      __builtin_amdgcn_raw_buffer_store_b128(gr, rws, pair * 16, MID_OFF + ((mat * 56 + piece) * 8 + m) * 512, AUX_SC1);
    }
  }
  v4u gg, gu;
  {
    const int p2 = 2 * grp + rh;
    int vo = (l >> 3) * 512 + (8 * w + c8) * 16;
    for (unsigned spins = 0;; ++spins) {
      asm volatile("" : "+v"(vo) :: "memory");
      gg = __builtin_amdgcn_raw_buffer_load_b128(rws, vo, MID_OFF + p2 * 4096, AUX_SC1);
      gu = __builtin_amdgcn_raw_buffer_load_b128(rws, vo, MID_OFF + (56 + p2) * 4096, AUX_SC1);
      const bool ok = (gg.z == tag0) & (gg.w == tag1) & (gu.z == tag0) & (gu.w == tag1);
      if (__all(ok) || spins > SPIN_CAP) break;
    }
  }
  STAMP(3);
  {
    float G = swap32_add(__uint_as_float(gg.x), __uint_as_float(gg.y));
    float U = swap32_add(__uint_as_float(gu.x), __uint_as_float(gu.y));
    G = swap16_add(G, G); U = swap16_add(U, U);
    G = fadd_ror8(G); U = fadd_ror8(U);
    G = rbf(G); U = rbf(U);
    const float inner = 0.7978845608028654f * (G + 0.044715f * G * G * G);
    const float e = __expf(-2.f * inner);
    const float act = rbf(G * __builtin_amdgcn_rcpf(1.f + e));
    const __hip_bfloat16 hb = __float2bfloat16(act * U);
    if ((l & 24) == 0) hs[16 * w + 2 * c8 + (l >> 5)] = __builtin_bit_cast(unsigned short, hb);
  }
  __syncthreads();
  const v4u hv = *(const v4u*)&hs[8 * q];
  v4f o0 = {0.f, 0.f, 0.f, 0.f};
#pragma unroll
  for (int s = 0; s < 8; ++s) {
    v4u fa;
#pragma unroll
    for (int r = 0; r < 4; ++r) fa[r] = __builtin_amdgcn_perm(0u, hv[s >> 1], sel[s & 1][r]);
    o0 = mfma(fa, dn[s], o0);
  }
  {
    float v0 = ror8_hi(o0[0]), v1 = ror8_hi(o0[1]), v2 = ror8_hi(o0[2]), v3 = ror8_hi(o0[3]);
    const float s0 = swap32_add(v0, v2), s1 = swap32_add(v1, v3);
    if (n < 8) {
      v4u gr = {__float_as_uint(s0), __float_as_uint(s1), tag0, tag1};
      const int pair = 4 * n + 2 * (g & 1) + (g >> 1);
      __builtin_amdgcn_raw_buffer_store_b128(gr, rws, pair * 16, FIN_OFF + ((4 * cq + w) * 56 + 2 * grp + rh) * 512, AUX_SC1);
    }
  }
  STAMP(4);
  if (w == 0 && b < 64) {
    v4u fv[7];
    int vo = (l >> 3) * 512 + (8 * (b & 3) + c8) * 16;
    const int so = FIN_OFF + (b >> 2) * 56 * 512;
    for (unsigned spins = 0;; ++spins) {
      bool ok = true;
      asm volatile("" : "+v"(vo) :: "memory");
#pragma unroll
      for (int i = 0; i < 7; ++i) fv[i] = __builtin_amdgcn_raw_buffer_load_b128(rws, vo, so + i * 4096, AUX_SC1);
#pragma unroll
      for (int i = 0; i < 7; ++i) ok &= (fv[i].z == tag0) & (fv[i].w == tag1);
      if (__all(ok) || spins > SPIN_CAP) break;
    }
    STAMP(5);
    float s0 = 0.f, s1 = 0.f;
#pragma unroll
    for (int i = 0; i < 7; ++i) { s0 += __uint_as_float(fv[i].x); s1 += __uint_as_float(fv[i].y); }
    float S = swap32_add(s0, s1);
    S = swap16_add(S, S);
    S = fadd_ror8(S);
    const unsigned short ob = __builtin_bit_cast(unsigned short, __float2bfloat16(S));
    if ((l & 24) == 0) ((unsigned short*)out)[16 * b + 2 * c8 + (l >> 5)] = ob;
    if (b == 0 && l == 0) {
      v2u ne = {tag0, tag1};
      __builtin_amdgcn_raw_buffer_store_b64(ne, rws, EPOCH_OFF, 0, AUX_SC1);
    }
    STAMP(6);
  }
}

extern "C" void kernel_launch(void* const* d_in, const int* in_sizes, int n_in, void* d_out, int out_size, void* d_ws,
                              size_t ws_size, hipStream_t stream) {
  geglu_fused<<<NWG, 256, 0, stream>>>((const unsigned short*)d_in[0], (const unsigned short*)d_in[1], (const unsigned short*)d_in[2],
                                       (const unsigned short*)d_in[3], (unsigned*)d_out, (unsigned char*)d_ws);
}
```

```cpp
#include <hip/hip_runtime.h>
__global__ void __launch_bounds__(256, 2)
geglu_fused(const void* x, const void* wg, const void* wu, const void* wd, void* out, void* ws) {
  __shared__ __attribute__((aligned(16))) unsigned short hs[64];
#if defined(__HIP_DEVICE_COMPILE__)
  const unsigned long long karg = (unsigned long long)__builtin_amdgcn_kernarg_segment_ptr();
  const unsigned bid = blockIdx.x, tid = threadIdx.x;
  asm volatile(
    "s_load_dwordx8 s[4:11], s[0:1], 0x0\n"
    "s_load_dwordx4 s[12:15], s[0:1], 0x20\n"
    "v_readfirstlane_b32 s3, v0\n"
    "v_and_b32 v1, 63, v0\n"
    "s_cmp_ge_u32 s2, 224\n"
    "s_cbranch_scc1 L%=_init\n"
    "s_lshr_b32 s3, s3, 6\n"
    "s_lshr_b32 s20, s2, 2\n"
    "s_and_b32 s21, s2, 3\n"
    "s_lshl_b32 s22, s3, 1\n"
    "s_add_u32 s22, s22, s20\n"
    "s_sub_u32 s23, s22, 56\n"
    "s_cmp_ge_u32 s22, 56\n"
    "s_cselect_b32 s22, s23, s22\n"
    "s_add_u32 s23, s21, s20\n"
    "s_and_b32 s23, s23, 3\n"
    "s_lshl_b32 s24, s21, 1\n"
    "s_and_b32 s25, s3, 1\n"
    "s_or_b32 s24, s24, s25\n"
    "s_mul_i32 s25, s24, 0xe0000\n"
    "s_lshl_b32 s26, s22, 7\n"
    "s_add_u32 s25, s25, s26\n"
    "v_bfe_u32 v2, v0, 3, 1\n"
    "v_bfe_u32 v3, v0, 4, 2\n"
    "v_lshl_or_b32 v5, v2, 2, v3\n"
    "v_and_b32 v6, 7, v0\n"
    "v_lshlrev_b32 v38, 4, v6\n"
    "v_mul_u32_u24 v2, 0x1c00, v5\n"
    "v_add3_u32 v2, v2, v38, s25\n"
    "s_mov_b32 s18, 0x700000\n"
    "s_mov_b32 s19, 0x20000\n"
    "s_mov_b32 s41, 0xe000\n"
    "s_mov_b32 s42, 0x1c000\n"
    "s_mov_b32 s43, 0x2a000\n"
    "s_mov_b32 s44, 0x38000\n"
    "s_mov_b32 s45, 0x46000\n"
    "s_mov_b32 s46, 0x54000\n"
    "s_mov_b32 s47, 0x62000\n"
    "s_mov_b32 s48, 0x70000\n"
    "s_mov_b32 s49, 0x7e000\n"
    "s_mov_b32 s50, 0x8c000\n"
    "s_mov_b32 s51, 0x9a000\n"
    "s_mov_b32 s52, 0xa8000\n"
    "s_mov_b32 s53, 0xb6000\n"
    "s_mov_b32 s54, 0xc4000\n"
    "s_mov_b32 s55, 0xd2000\n"
    "s_lshl_b32 s26, s24, 8\n"
    "v_lshl_add_u32 v4, v1, 2, s26\n"
    "s_cmp_lt_u32 s3, 2\n"
    "s_waitcnt lgkmcnt(0)\n"
    "s_cselect_b64 s[16:17], s[6:7], s[8:9]\n"
    "global_load_dword v4, v4, s[4:5]\n"
    "buffer_load_dwordx4 v[40:43], v2, s[16:19], 0 offen nt\n"
    "buffer_load_dwordx4 v[44:47], v2, s[16:19], s41 offen nt\n"
    "buffer_load_dwordx4 v[48:51], v2, s[16:19], s42 offen nt\n"
    "buffer_load_dwordx4 v[52:55], v2, s[16:19], s43 offen nt\n"
    "buffer_load_dwordx4 v[56:59], v2, s[16:19], s44 offen nt\n"
    "buffer_load_dwordx4 v[60:63], v2, s[16:19], s45 offen nt\n"
    "buffer_load_dwordx4 v[64:67], v2, s[16:19], s46 offen nt\n"
    "buffer_load_dwordx4 v[68:71], v2, s[16:19], s47 offen nt\n"
    "s_load_dwordx2 s[76:77], s[14:15], 0x0\n"
    "s_mov_b64 s[28:29], s[10:11]\n"
    "s_mov_b32 s30, 0x700000\n"
    "s_mov_b32 s31, 0x20000\n"
    "s_lshl_b32 s26, s20, 17\n"
    "s_lshl_b32 s27, s23, 9\n"
    "s_add_u32 s26, s26, s27\n"
    "s_lshl_b32 s27, s3, 7\n"
    "s_add_u32 s26, s26, s27\n"
    "v_lshlrev_b32 v3, 14, v5\n"
    "v_add3_u32 v3, v3, v38, s26\n"
    "s_mov_b64 exec, 1\n"
    "v_mov_b32 v170, 0\n"
    "buffer_load_dword v171, v3, s[28:31], 0 offen nt\n"
    "global_load_dword v171, v170, s[14:15] offset:256\n"
    "s_mov_b64 exec, -1\n"
    "s_movk_i32 s57, 0x1000\n"
    "s_movk_i32 s58, 0x2000\n"
    "s_movk_i32 s59, 0x3000\n"
    "s_mov_b64 s[36:37], s[14:15]\n"
    "s_mov_b32 s38, 0x7ac00\n"
    "s_mov_b32 s39, 0x20000\n"
    "v_and_b32 v5, 1, v0\n"
    "v_bfe_u32 v7, v0, 5, 1\n"
    "v_lshl_or_b32 v5, v5, 1, v7\n"
    "v_lshlrev_b32 v5, 2, v5\n"
    "v_mov_b32 v39, v6\n"
    "v_bfe_u32 v7, v0, 4, 1\n"
    "v_mul_u32_u24 v7, 0x202, v7\n"
    "v_add_u32 v7, 0x100, v7\n"
    "v_xor_b32 v7, 0xc0c, v7\n"
    "v_and_b32 v8, 2, v0\n"
    "v_lshlrev_b32 v8, 3, v8\n"
    "v_lshlrev_b32 v7, v8, v7\n"
    "v_xor_b32 v10, 0xc0c0c0c, v7\n"
    "v_mov_b32 v11, 0xc0c0c0c\n"
    "v_bfe_u32 v12, v0, 2, 2\n"
    "v_cmp_eq_u32 vcc, 0, v12\n"
    "s_nop 1\n"
    "v_cndmask_b32 v6, v11, v10, vcc\n"
    "v_cmp_eq_u32 vcc, 1, v12\n"
    "s_nop 1\n"
    "v_cndmask_b32 v7, v11, v10, vcc\n"
    "v_cmp_eq_u32 vcc, 2, v12\n"
    "s_nop 1\n"
    "v_cndmask_b32 v8, v11, v10, vcc\n"
    "v_cmp_eq_u32 vcc, 3, v12\n"
    "s_nop 1\n"
    "v_cndmask_b32 v9, v11, v10, vcc\n"
    "v_lshrrev_b32 v136, 3, v1\n"
    "v_lshlrev_b32 v137, 7, v136\n"
    "v_lshl_or_b32 v137, v39, 3, v137\n"
    "s_lshl_b32 s26, s23, 2\n"
    "s_add_u32 s26, s26, s3\n"
    "s_lshl_b32 s27, s26, 10\n"
    "s_add_u32 s27, s27, 4096\n"
    "v_add_u32 v138, s27, v137\n"
    "s_lshl_b32 s27, s26, 7\n"
    "v_lshl_add_u32 v139, v39, 3, v136\n"
    "v_lshl_add_u32 v139, v139, 1, s27\n"
    "s_lshr_b32 s26, s3, 1\n"
    "s_mul_i32 s26, s26, 56\n"
    "s_add_u32 s26, s26, s22\n"
    "v_and_b32 v140, 1, v0\n"
    "v_lshlrev_b32 v140, 6, v140\n"
    "v_and_or_b32 v140, v0, 48, v140\n"
    "v_mov_b32 v142, 0\n"
    "v_mov_b32 v143, 0x43380000\n"
    "v_mov_b32 v144, 0x43300000\n"
    "v_mov_b32 v145, 0xc0135761\n"
    "s_mov_b32 s34, 0\n"
    "s_mov_b32 s35, 0x42c00000\n"
    "s_mov_b32 s82, 0\n"
    "s_mov_b32 s83, 0x3d200000\n"
    "v_mov_b32 v146, 0\n"
    "v_mov_b32 v147, 0xc0670000\n"
    "s_mov_b32 s84, 0xfffff\n"
    "s_mov_b32 s85, 0xe0000000\n"
    "s_mov_b32 s86, 0x05040100\n"
    "s_mov_b32 s87, 0x07060302\n"
    "s_lshl_b32 s26, s26, 12\n"
    "s_lshl_b32 s27, s24, 9\n"
    "s_add_u32 s26, s26, s27\n"
    "s_add_u32 s26, s26, 20480\n"
    "v_lshl_add_u32 v141, v1, 3, s26\n"
    "v_lshlrev_b32 v148, 9, v136\n"
    "s_lshl_b32 s26, s20, 12\n"
    "s_lshl_b32 s27, s3, 7\n"
    "s_add_u32 s26, s26, s27\n"
    "s_add_u32 s26, s26, 20480\n"
    "v_add3_u32 v148, v148, v38, s26\n"
    "s_mov_b32 s88, 0x38000\n"
    "v_bfe_u32 v149, v0, 5, 1\n"
    "v_lshl_or_b32 v149, v39, 1, v149\n"
    "v_and_b32 v150, 7, v149\n"
    "v_lshrrev_b32 v149, 3, v149\n"
    "v_lshl_add_u32 v149, v150, 3, v149\n"
    "s_lshl_b32 s27, s3, 2\n"
    "v_lshl_add_u32 v149, v149, 1, s27\n"
    "s_waitcnt vmcnt(10)\n"
    "ds_bpermute_b32 v10, v5, v4 offset:0\n"
    "ds_bpermute_b32 v11, v5, v4 offset:16\n"
    "ds_bpermute_b32 v12, v5, v4 offset:32\n"
    "ds_bpermute_b32 v13, v5, v4 offset:48\n"
    "ds_bpermute_b32 v14, v5, v4 offset:64\n"
    "ds_bpermute_b32 v15, v5, v4 offset:80\n"
    "ds_bpermute_b32 v16, v5, v4 offset:96\n"
    "ds_bpermute_b32 v17, v5, v4 offset:112\n"
    "ds_bpermute_b32 v18, v5, v4 offset:128\n"
    "ds_bpermute_b32 v19, v5, v4 offset:144\n"
    "ds_bpermute_b32 v20, v5, v4 offset:160\n"
    "ds_bpermute_b32 v21, v5, v4 offset:176\n"
    "ds_bpermute_b32 v22, v5, v4 offset:192\n"
    "ds_bpermute_b32 v23, v5, v4 offset:208\n"
    "ds_bpermute_b32 v24, v5, v4 offset:224\n"
    "ds_bpermute_b32 v25, v5, v4 offset:240\n"
    "s_waitcnt vmcnt(9)\n"
    "buffer_load_dwordx4 v[72:75], v2, s[16:19], s48 offen nt\n"
    "s_waitcnt lgkmcnt(0)\n"
    "s_add_u32 s76, s76, 1\n"
    "s_add_u32 s77, s77, 1\n"
    "v_mov_b32 v154, s76\n"
    "v_mov_b32 v155, s77\n"
    "v_and_b32 v160, 1, v0\n"
    "v_cmp_eq_u32 vcc, 1, v160\n"
    "s_nop 1\n"
    "v_cndmask_b32 v155, v154, v155, vcc\n"
    "v_perm_b32 v26, v10, v10, v6\n"
    "v_perm_b32 v27, v10, v10, v7\n"
    "v_perm_b32 v28, v10, v10, v8\n"
    "v_perm_b32 v29, v10, v10, v9\n"
    "s_nop 1\n"
    "v_mfma_f32_16x16x32_bf16 v[34:37], v[26:29], v[40:43], 0\n"
    "v_perm_b32 v30, v11, v11, v6\n"
    "v_perm_b32 v31, v11, v11, v7\n"
    "v_perm_b32 v32, v11, v11, v8\n"
    "v_perm_b32 v33, v11, v11, v9\n"
    "s_waitcnt vmcnt(9)\n"
    "buffer_load_dwordx4 v[76:79], v2, s[16:19], s49 offen nt\n"
    "v_mfma_f32_16x16x32_bf16 v[34:37], v[30:33], v[44:47], v[34:37]\n"
    "v_perm_b32 v26, v12, v12, v6\n"
    "v_perm_b32 v27, v12, v12, v7\n"
    "v_perm_b32 v28, v12, v12, v8\n"
    "v_perm_b32 v29, v12, v12, v9\n"
    "s_waitcnt vmcnt(9)\n"
    "buffer_load_dwordx4 v[80:83], v2, s[16:19], s50 offen nt\n"
    "v_mfma_f32_16x16x32_bf16 v[34:37], v[26:29], v[48:51], v[34:37]\n"
    "v_perm_b32 v30, v13, v13, v6\n"
    "v_perm_b32 v31, v13, v13, v7\n"
    "v_perm_b32 v32, v13, v13, v8\n"
    "v_perm_b32 v33, v13, v13, v9\n"
    "s_waitcnt vmcnt(9)\n"
    "buffer_load_dwordx4 v[84:87], v2, s[16:19], s51 offen nt\n"
    "v_mfma_f32_16x16x32_bf16 v[34:37], v[30:33], v[52:55], v[34:37]\n"
    "v_perm_b32 v26, v14, v14, v6\n"
    "v_perm_b32 v27, v14, v14, v7\n"
    "v_perm_b32 v28, v14, v14, v8\n"
    "v_perm_b32 v29, v14, v14, v9\n"
    "s_waitcnt vmcnt(9)\n"
    "buffer_load_dwordx4 v[88:91], v2, s[16:19], s52 offen nt\n"
    "v_mfma_f32_16x16x32_bf16 v[34:37], v[26:29], v[56:59], v[34:37]\n"
    "v_perm_b32 v30, v15, v15, v6\n"
    "v_perm_b32 v31, v15, v15, v7\n"
    "v_perm_b32 v32, v15, v15, v8\n"
    "v_perm_b32 v33, v15, v15, v9\n"
    "s_waitcnt vmcnt(9)\n"
    "buffer_load_dwordx4 v[92:95], v2, s[16:19], s53 offen nt\n"
    "v_mfma_f32_16x16x32_bf16 v[34:37], v[30:33], v[60:63], v[34:37]\n"
    "v_perm_b32 v26, v16, v16, v6\n"
    "v_perm_b32 v27, v16, v16, v7\n"
    "v_perm_b32 v28, v16, v16, v8\n"
    "v_perm_b32 v29, v16, v16, v9\n"
    "s_waitcnt vmcnt(9)\n"
    "buffer_load_dwordx4 v[96:99], v2, s[16:19], s54 offen nt\n"
    "v_mfma_f32_16x16x32_bf16 v[34:37], v[26:29], v[64:67], v[34:37]\n"
    "v_perm_b32 v30, v17, v17, v6\n"
    "v_perm_b32 v31, v17, v17, v7\n"
    "v_perm_b32 v32, v17, v17, v8\n"
    "v_perm_b32 v33, v17, v17, v9\n"
    "s_waitcnt vmcnt(9)\n"
    "buffer_load_dwordx4 v[100:103], v2, s[16:19], s55 offen nt\n"
    "v_mfma_f32_16x16x32_bf16 v[34:37], v[30:33], v[68:71], v[34:37]\n"
    "v_perm_b32 v26, v18, v18, v6\n"
    "v_perm_b32 v27, v18, v18, v7\n"
    "v_perm_b32 v28, v18, v18, v8\n"
    "v_perm_b32 v29, v18, v18, v9\n"
    "s_waitcnt vmcnt(7)\n"
    "buffer_load_dwordx4 v[104:107], v3, s[28:31], 0 offen nt\n"
    "v_mfma_f32_16x16x32_bf16 v[34:37], v[26:29], v[72:75], v[34:37]\n"
    "v_perm_b32 v30, v19, v19, v6\n"
    "v_perm_b32 v31, v19, v19, v7\n"
    "v_perm_b32 v32, v19, v19, v8\n"
    "v_perm_b32 v33, v19, v19, v9\n"
    "s_waitcnt vmcnt(7)\n"
    "buffer_load_dwordx4 v[108:111], v3, s[28:31], 0 offen offset:2048 nt\n"
    "v_mfma_f32_16x16x32_bf16 v[34:37], v[30:33], v[76:79], v[34:37]\n"
    "v_perm_b32 v26, v20, v20, v6\n"
    "v_perm_b32 v27, v20, v20, v7\n"
    "v_perm_b32 v28, v20, v20, v8\n"
    "v_perm_b32 v29, v20, v20, v9\n"
    "s_waitcnt vmcnt(7)\n"
    "buffer_load_dwordx4 v[112:115], v3, s[28:31], s57 offen nt\n"
    "v_mfma_f32_16x16x32_bf16 v[34:37], v[26:29], v[80:83], v[34:37]\n"
    "v_perm_b32 v30, v21, v21, v6\n"
    "v_perm_b32 v31, v21, v21, v7\n"
    "v_perm_b32 v32, v21, v21, v8\n"
    "v_perm_b32 v33, v21, v21, v9\n"
    "s_waitcnt vmcnt(7)\n"
    "buffer_load_dwordx4 v[116:119], v3, s[28:31], s57 offen offset:2048 nt\n"
    "v_mfma_f32_16x16x32_bf16 v[34:37], v[30:33], v[84:87], v[34:37]\n"
    "v_perm_b32 v26, v22, v22, v6\n"
    "v_perm_b32 v27, v22, v22, v7\n"
    "v_perm_b32 v28, v22, v22, v8\n"
    "v_perm_b32 v29, v22, v22, v9\n"
    "s_waitcnt vmcnt(7)\n"
    "buffer_load_dwordx4 v[120:123], v3, s[28:31], s58 offen nt\n"
    "v_mfma_f32_16x16x32_bf16 v[34:37], v[26:29], v[88:91], v[34:37]\n"
    "v_perm_b32 v30, v23, v23, v6\n"
    "v_perm_b32 v31, v23, v23, v7\n"
    "v_perm_b32 v32, v23, v23, v8\n"
    "v_perm_b32 v33, v23, v23, v9\n"
    "s_waitcnt vmcnt(7)\n"
    "buffer_load_dwordx4 v[124:127], v3, s[28:31], s58 offen offset:2048 nt\n"
    "v_mfma_f32_16x16x32_bf16 v[34:37], v[30:33], v[92:95], v[34:37]\n"
    "v_perm_b32 v26, v24, v24, v6\n"
    "v_perm_b32 v27, v24, v24, v7\n"
    "v_perm_b32 v28, v24, v24, v8\n"
    "v_perm_b32 v29, v24, v24, v9\n"
    "s_waitcnt vmcnt(7)\n"
    "buffer_load_dwordx4 v[128:131], v3, s[28:31], s59 offen nt\n"
    "v_mfma_f32_16x16x32_bf16 v[34:37], v[26:29], v[96:99], v[34:37]\n"
    "v_perm_b32 v30, v25, v25, v6\n"
    "v_perm_b32 v31, v25, v25, v7\n"
    "v_perm_b32 v32, v25, v25, v8\n"
    "v_perm_b32 v33, v25, v25, v9\n"
    "s_waitcnt vmcnt(7)\n"
    "buffer_load_dwordx4 v[132:135], v3, s[28:31], s59 offen offset:2048 nt\n"
    "v_mfma_f32_16x16x32_bf16 v[34:37], v[30:33], v[100:103], v[34:37]\n"
    "s_nop 7\n"
    "v_add_f32_dpp v154, v35, v34 row_ror:8 row_mask:0xf bank_mask:0x3\n"
    "v_add_f32_dpp v154, v36, v37 row_ror:8 row_mask:0xf bank_mask:0xc\n"
    "global_store_dwordx2 v141, v[154:155], s[14:15] sc1\n"
    "buffer_load_dwordx4 v[156:159], off, s[36:39], 0 offset:1024 sc1\n"
    "s_waitcnt vmcnt(8)\n"
    "v_perm_b32 v40, v108, v104, s86\n"
    "v_perm_b32 v44, v108, v104, s87\n"
    "v_perm_b32 v48, v109, v105, s86\n"
    "v_perm_b32 v52, v109, v105, s87\n"
    "v_perm_b32 v56, v110, v106, s86\n"
    "v_perm_b32 v60, v110, v106, s87\n"
    "v_perm_b32 v64, v111, v107, s86\n"
    "v_perm_b32 v68, v111, v107, s87\n"
    "s_waitcnt vmcnt(6)\n"
    "v_perm_b32 v41, v116, v112, s86\n"
    "v_perm_b32 v45, v116, v112, s87\n"
    "v_perm_b32 v49, v117, v113, s86\n"
    "v_perm_b32 v53, v117, v113, s87\n"
    "v_perm_b32 v57, v118, v114, s86\n"
    "v_perm_b32 v61, v118, v114, s87\n"
    "v_perm_b32 v65, v119, v115, s86\n"
    "v_perm_b32 v69, v119, v115, s87\n"
    "s_waitcnt vmcnt(4)\n"
    "v_perm_b32 v42, v124, v120, s86\n"
    "v_perm_b32 v46, v124, v120, s87\n"
    "v_perm_b32 v50, v125, v121, s86\n"
    "v_perm_b32 v54, v125, v121, s87\n"
    "v_perm_b32 v58, v126, v122, s86\n"
    "v_perm_b32 v62, v126, v122, s87\n"
    "v_perm_b32 v66, v127, v123, s86\n"
    "v_perm_b32 v70, v127, v123, s87\n"
    "s_waitcnt vmcnt(2)\n"
    "s_barrier\n"
    "buffer_load_dwordx4 v[160:163], v148, s[36:39], 0 offen sc1\n"
    "buffer_load_dwordx4 v[164:167], v148, s[36:39], s88 offen sc1\n"
    "v_perm_b32 v43, v132, v128, s86\n"
    "v_perm_b32 v47, v132, v128, s87\n"
    "v_perm_b32 v51, v133, v129, s86\n"
    "v_perm_b32 v55, v133, v129, s87\n"
    "v_perm_b32 v59, v134, v130, s86\n"
    "v_perm_b32 v63, v134, v130, s87\n"
    "v_perm_b32 v67, v135, v131, s86\n"
    "v_perm_b32 v71, v135, v131, s87\n"
    "s_mov_b32 s93, 0\n"
    "L%=_poll:\n"
    "s_waitcnt vmcnt(0)\n"
    "v_cmp_eq_u32 vcc, s76, v161\n"
    "v_cmp_eq_u32 s[94:95], s77, v163\n"
    "v_cmp_eq_u32 s[96:97], s76, v165\n"
    "v_cmp_eq_u32 s[98:99], s77, v167\n"
    "s_and_b64 vcc, vcc, s[94:95]\n"
    "s_and_b64 s[96:97], s[96:97], s[98:99]\n"
    "v_cmp_eq_u32 s[94:95], s76, v157\n"
    "v_cmp_eq_u32 s[98:99], s77, v159\n"
    "s_and_b64 vcc, vcc, s[96:97]\n"
    "s_and_b64 s[94:95], s[94:95], s[98:99]\n"
    "s_and_b64 vcc, vcc, s[94:95]\n"
    "s_cmp_eq_u64 vcc, exec\n"
    "s_cbranch_scc1 L%=_got\n"
    "buffer_load_dwordx4 v[160:163], v148, s[36:39], 0 offen sc1\n"
    "buffer_load_dwordx4 v[164:167], v148, s[36:39], s88 offen sc1\n"
    "buffer_load_dwordx4 v[156:159], off, s[36:39], 0 offset:1024 sc1\n"
    "s_add_u32 s93, s93, 1\n"
    "s_cmp_lt_u32 s93, 0x20000\n"
    "s_cbranch_scc1 L%=_poll\n"
    "s_waitcnt vmcnt(0)\n"
    "L%=_got:\n"
    "v_permlane32_swap_b32 v160, v162\n"
    "v_permlane32_swap_b32 v164, v166\n"
    "v_add_f32 v28, v160, v162\n"
    "v_add_f32 v29, v164, v166\n"
    "v_mov_b32 v30, v28\n"
    "v_mov_b32 v31, v29\n"
    "s_nop 0\n"
    "v_permlane16_swap_b32 v28, v30\n"
    "v_permlane16_swap_b32 v29, v31\n"
    "v_add_f32 v28, v28, v30\n"
    "v_add_f32 v29, v29, v31\n"
    "s_nop 0\n"
    "v_add_f32_dpp v28, v28, v28 row_ror:8 row_mask:0xf bank_mask:0xf\n"
    "v_add_f32_dpp v29, v29, v29 row_ror:8 row_mask:0xf bank_mask:0xf\n"
    "v_cvt_pk_bf16_f32 v29, v28, v29\n"
    "v_lshlrev_b32 v28, 16, v29\n"
    "v_and_b32 v29, 0xffff0000, v29\n"
    "v_mul_f32 v32, v28, v28\n"
    "v_mov_b32 v33, v145\n"
    "v_fmac_f32 v33, 0xbdd2d3e7, v32\n"
    "v_mul_f32 v32, v33, v28\n"
    "v_exp_f32 v32, v32\n"
    "s_nop 0\n"
    "v_add_f32 v32, 1.0, v32\n"
    "v_rcp_f32 v32, v32\n"
    "s_nop 0\n"
    "v_mul_f32 v32, v32, v28\n"
    "v_cvt_pk_bf16_f32 v32, v32, v32\n"
    "v_and_b32 v32, 0xffff0000, v32\n"
    "v_mul_f32 v32, v32, v29\n"
    "v_cvt_pk_bf16_f32 v32, v32, v32\n"
    "ds_write_b16 v149, v32\n"
    "s_waitcnt lgkmcnt(0)\n"
    "s_barrier\n"
    "ds_read_b128 v[26:29], v140\n"
    "s_waitcnt lgkmcnt(0)\n"
    "v_mfma_f32_16x16x32_bf16 v[72:75], v[26:29], v[40:43], 0\n"
    "v_mfma_f32_16x16x32_bf16 v[76:79], v[26:29], v[44:47], 0\n"
    "v_mfma_f32_16x16x32_bf16 v[80:83], v[26:29], v[48:51], 0\n"
    "v_mfma_f32_16x16x32_bf16 v[84:87], v[26:29], v[52:55], 0\n"
    "v_mfma_f32_16x16x32_bf16 v[88:91], v[26:29], v[56:59], 0\n"
    "v_mfma_f32_16x16x32_bf16 v[92:95], v[26:29], v[60:63], 0\n"
    "v_mfma_f32_16x16x32_bf16 v[96:99], v[26:29], v[64:67], 0\n"
    "v_mfma_f32_16x16x32_bf16 v[100:103], v[26:29], v[68:71], 0\n"
    "s_nop 0\n"
    "v_add_f32_dpp v30, v73, v72 row_ror:8 row_mask:0x1 bank_mask:0x3\n"
    "v_add_f32_dpp v30, v76, v77 row_ror:8 row_mask:0x1 bank_mask:0xc\n"
    "v_add_f32_dpp v30, v81, v80 row_ror:8 row_mask:0x2 bank_mask:0x3\n"
    "v_add_f32_dpp v30, v84, v85 row_ror:8 row_mask:0x2 bank_mask:0xc\n"
    "v_add_f32_dpp v30, v89, v88 row_ror:8 row_mask:0x4 bank_mask:0x3\n"
    "v_add_f32_dpp v30, v92, v93 row_ror:8 row_mask:0x4 bank_mask:0xc\n"
    "v_add_f32_dpp v30, v97, v96 row_ror:8 row_mask:0x8 bank_mask:0x3\n"
    "v_add_f32_dpp v30, v100, v101 row_ror:8 row_mask:0x8 bank_mask:0xc\n"
    "v_cvt_f64_f32 v[30:31], v30\n"
    "v_fma_f64 v[30:31], v[30:31], s[34:35], v[142:143]\n"
    "v_add_u32 v31, 0xc0c82000, v31\n"
    "global_atomic_add_x2 v[32:33], v138, v[30:31], s[14:15] sc0\n"
    "s_waitcnt vmcnt(0)\n"
    "v_lshl_add_u64 v[30:31], v[30:31], 0, v[32:33]\n"
    "v_cmp_le_u32 vcc, s85, v31\n"
    "v_and_or_b32 v31, v31, s84, v144\n"
    "v_fma_f64 v[30:31], v[30:31], s[82:83], v[146:147]\n"
    "s_and_b64 exec, exec, vcc\n"
    "s_cbranch_execz L%=_done\n"
    "v_cvt_f32_f64 v30, v[30:31]\n"
    "v_cvt_pk_bf16_f32 v30, v30, v30\n"
    "global_store_short v139, v30, s[12:13] sc1\n"
    "v_cmp_eq_u32 vcc, 0, v139\n"
    "s_and_b64 exec, exec, vcc\n"
    "s_cbranch_execz L%=_done\n"
    "v_mov_b32 v30, s76\n"
    "v_mov_b32 v31, s77\n"
    "global_store_dwordx2 v139, v[30:31], s[14:15] sc1\n"
    "L%=_done:\n"
    "s_endpgm\n"
    "L%=_init:\n"
    "s_sub_u32 s20, s2, 224\n"
    "v_lshrrev_b32 v2, 2, v0\n"
    "v_lshlrev_b32 v2, 7, v2\n"
    "v_and_b32 v3, 3, v0\n"
    "v_lshl_or_b32 v2, v3, 4, v2\n"
    "s_lshl_b32 s21, s20, 14\n"
    "s_add_u32 s21, s21, 4096\n"
    "v_add_u32 v2, s21, v2\n"
    "v_add_u32 v3, 0x2000, v2\n"
    "v_mov_b32 v4, 0\n"
    "v_mov_b32 v5, 0\n"
    "v_mov_b32 v6, 0\n"
    "v_mov_b32 v7, 0\n"
    "s_waitcnt lgkmcnt(0)\n"
    "global_store_dwordx4 v2, v[4:7], s[14:15] sc1\n"
    "global_store_dwordx4 v3, v[4:7], s[14:15] sc1\n"
    "s_load_dwordx2 s[76:77], s[14:15], 0x0\n"
    "s_lshl_b32 s21, s20, 4\n"
    "s_add_u32 s21, s21, 1024\n"
    "v_mov_b32 v2, s21\n"
    "s_waitcnt lgkmcnt(0)\n"
    "s_waitcnt vmcnt(0)\n"
    "s_barrier\n"
    "s_add_u32 s76, s76, 1\n"
    "s_add_u32 s77, s77, 1\n"
    "v_cmp_eq_u32 vcc, 0, v0\n"
    "s_and_b64 exec, exec, vcc\n"
    "s_cbranch_execz L%=_end\n"
    "v_mov_b32 v5, s76\n"
    "v_mov_b32 v7, s77\n"
    "global_store_dwordx4 v2, v[4:7], s[14:15] sc1\n"
    "L%=_end:\n"
    :: "{s[0:1]}"(karg), "{s2}"(bid), "{v0}"(tid), "s"((unsigned)(unsigned long long)(__attribute__((address_space(3))) void*)hs)
    : "v1", "v2", "v3", "v4", "v5", "v6", "v7", "v8", "v9", "v10", "v11", "v12", "v13", "v14", "v15", "v16", "v17", "v18", "v19", "v20", "v21", "v22", "v23", "v24", "v25", "v26", "v27", "v28", "v29", "v30", "v31", "v32", "v33", "v34", "v35", "v36", "v37", "v38", "v39", "v40", "v41", "v42", "v43", "v44", "v45", "v46", "v47", "v48", "v49", "v50", "v51", "v52", "v53", "v54", "v55", "v56", "v57", "v58", "v59", "v60", "v61", "v62", "v63", "v64", "v65", "v66", "v67", "v68", "v69", "v70", "v71", "v72", "v73", "v74", "v75", "v76", "v77", "v78", "v79", "v80", "v81", "v82", "v83", "v84", "v85", "v86", "v87", "v88", "v89", "v90", "v91", "v92", "v93", "v94", "v95", "v96", "v97", "v98", "v99", "v100", "v101", "v102", "v103", "v104", "v105", "v106", "v107", "v108", "v109", "v110", "v111", "v112", "v113", "v114", "v115", "v116", "v117", "v118", "v119", "v120", "v121", "v122", "v123", "v124", "v125", "v126", "v127", "v128", "v129", "v130", "v131", "v132", "v133", "v134", "v135", "v136", "v137", "v138", "v139", "v140", "v141", "v142", "v143", "v144", "v145", "v146", "v147", "v148", "v149", "v150", "v151", "v152", "v153", "v154", "v155", "v156", "v157", "v158", "v159", "v160", "v161", "v162", "v163", "v164", "v165", "v166", "v167", "v168", "v169", "v170", "v171", "s3", "s4", "s5", "s6", "s7", "s8", "s9", "s10", "s11", "s12", "s13", "s14", "s15", "s16", "s17", "s18", "s19", "s20", "s21", "s22", "s23", "s24", "s25", "s26", "s27", "s28", "s29", "s30", "s31", "s34", "s35", "s36", "s37", "s38", "s39", "s41", "s42", "s43", "s44", "s45", "s46", "s47", "s48", "s49", "s50", "s51", "s52", "s53", "s54", "s55", "s56", "s57", "s58", "s59", "s60", "s61", "s62", "s63", "s64", "s65", "s66", "s67", "s68", "s69", "s70", "s71", "s72", "s73", "s74", "s75", "s76", "s77", "s78", "s79", "s80", "s81", "s82", "s83", "s84", "s85", "s86", "s87", "s88", "s89", "s90", "s91", "s92", "s93", "s94", "s95", "s96", "s97", "s98", "s99", "vcc", "scc", "memory");
#endif
}
extern "C" void kernel_launch(void* const* d_in, const int* in_sizes, int n_in, void* d_out, int out_size, void* d_ws,
                              size_t ws_size, hipStream_t stream) {
  geglu_fused<<<225, 256, 0, stream>>>(d_in[0], d_in[1], d_in[2], d_in[3], d_out, d_ws);
}
```

```cpp
#include <hip/hip_runtime.h>

__global__ void __launch_bounds__(256, 2) __attribute__((aligned(4096)))
geglu_fused(const void* x, const void* w_gate, const void* w_up, const void* w_down, void* out, void* ws) {
#if defined(__HIP_DEVICE_COMPILE__)
  __shared__ __attribute__((aligned(16))) unsigned short hs[128];
  const unsigned long long karg = (unsigned long long)__builtin_amdgcn_kernarg_segment_ptr();
  const unsigned bid = blockIdx.x, tid = threadIdx.x;
  asm volatile(
      "s_load_dwordx8 s[4:11], s[0:1], 0x0\n"
      "s_load_dwordx4 s[12:15], s[0:1], 0x20\n"
      "v_readfirstlane_b32 s3, v0\n"
      "v_and_b32 v1, 63, v0\n"
      "s_cmp_ge_u32 s2, 224\n"
      "s_cbranch_scc1 L%=_init\n"
      "s_lshr_b32 s3, s3, 6\n"
      "s_lshr_b32 s20, s2, 2\n"
      "s_lshl_b32 s22, s3, 1\n"
      "s_add_u32 s22, s22, s20\n"
      "s_sub_u32 s23, s22, 56\n"
      "s_cmp_ge_u32 s22, 56\n"
      "s_cselect_b32 s22, s23, s22\n"
      "s_and_b32 s24, s2, 3\n"
      "s_lshl_b32 s24, s24, 1\n"
      "s_and_b32 s25, s3, 1\n"
      "s_or_b32 s24, s24, s25\n"
      "s_mul_i32 s25, s24, 0xe0000\n"
      "s_lshl_b32 s26, s22, 7\n"
      "s_add_u32 s25, s25, s26\n"
      "v_bfe_u32 v2, v0, 3, 1\n"
      "v_bfe_u32 v3, v0, 4, 2\n"
      "v_lshl_or_b32 v5, v2, 2, v3\n"
      "v_and_b32 v39, 7, v0\n"
      "v_lshlrev_b32 v38, 4, v39\n"
      "v_mul_u32_u24 v2, 0x1c00, v5\n"
      "v_add3_u32 v2, v2, v38, s25\n"
      "s_mov_b32 s18, 0x700000\n"
      "s_mov_b32 s19, 0x20000\n"
      "s_mov_b32 s41, 0xe000\n"
      "s_mov_b32 s42, 0x1c000\n"
      "s_mov_b32 s43, 0x2a000\n"
      "s_mov_b32 s44, 0x38000\n"
      "s_mov_b32 s45, 0x46000\n"
      "s_mov_b32 s46, 0x54000\n"
      "s_mov_b32 s47, 0x62000\n"
      "s_mov_b32 s48, 0x70000\n"
      "s_mov_b32 s49, 0x7e000\n"
      "s_mov_b32 s50, 0x8c000\n"
      "s_mov_b32 s51, 0x9a000\n"
      "s_mov_b32 s52, 0xa8000\n"
      "s_mov_b32 s53, 0xb6000\n"
      "s_mov_b32 s54, 0xc4000\n"
      "s_mov_b32 s55, 0xd2000\n"
      "s_lshl_b32 s26, s24, 8\n"
      "v_lshl_add_u32 v4, v1, 2, s26\n"
      "s_cmp_lt_u32 s3, 2\n"
      "s_waitcnt lgkmcnt(0)\n"
      "s_cselect_b64 s[16:17], s[6:7], s[8:9]\n"
      "global_load_dword v4, v4, s[4:5]\n"
      "buffer_load_dwordx4 v[40:43], v2, s[16:19], 0 offen nt\n"
      "buffer_load_dwordx4 v[44:47], v2, s[16:19], s41 offen nt\n"
      "buffer_load_dwordx4 v[48:51], v2, s[16:19], s42 offen nt\n"
      "buffer_load_dwordx4 v[52:55], v2, s[16:19], s43 offen nt\n"
      "buffer_load_dwordx4 v[56:59], v2, s[16:19], s44 offen nt\n"
      "buffer_load_dwordx4 v[60:63], v2, s[16:19], s45 offen nt\n"
      "buffer_load_dwordx4 v[64:67], v2, s[16:19], s46 offen nt\n"
      "buffer_load_dwordx4 v[68:71], v2, s[16:19], s47 offen nt\n"
      "s_load_dwordx2 s[60:61], s[14:15], 0x0\n"
      "s_and_b32 s20, s2, 7\n"
      "s_mul_i32 s20, s20, 7\n"
      "s_lshr_b32 s23, s2, 5\n"
      "s_add_u32 s20, s20, s23\n"
      "s_bfe_u32 s23, s2, 0x20003\n"
      "s_and_b32 s26, s23, 1\n"
      "s_lshr_b32 s27, s23, 1\n"
      "s_lshl_b32 s27, s27, 3\n"
      "s_add_u32 s26, s26, s27\n"
      "s_lshl_b32 s27, s3, 1\n"
      "s_add_u32 s21, s26, s27\n"
      "s_mov_b64 s[28:29], s[10:11]\n"
      "s_mov_b32 s30, 0x700000\n"
      "s_mov_b32 s31, 0x20000\n"
      "s_lshl_b32 s26, s20, 17\n"
      "s_lshl_b32 s27, s21, 7\n"
      "s_add_u32 s26, s26, s27\n"
      "v_lshlrev_b32 v3, 14, v5\n"
      "v_add3_u32 v3, v3, v38, s26\n"
      "s_movk_i32 s57, 0x1000\n"
      "s_movk_i32 s58, 0x2000\n"
      "s_movk_i32 s59, 0x3000\n"
      "v_and_b32 v5, 1, v0\n"
      "v_bfe_u32 v7, v0, 5, 1\n"
      "v_lshl_or_b32 v5, v5, 1, v7\n"
      "v_lshlrev_b32 v5, 2, v5\n"
      "v_bfe_u32 v7, v0, 4, 1\n"
      "v_mul_u32_u24 v7, 0x202, v7\n"
      "v_add_u32 v7, 0x100, v7\n"
      "v_xor_b32 v7, 0xc0c, v7\n"
      "v_and_b32 v8, 2, v0\n"
      "v_lshlrev_b32 v8, 3, v8\n"
      "v_lshlrev_b32 v7, v8, v7\n"
      "v_xor_b32 v10, 0xc0c0c0c, v7\n"
      "v_mov_b32 v11, 0xc0c0c0c\n"
      "v_bfe_u32 v12, v0, 2, 2\n"
      "v_cmp_eq_u32 s[62:63], 0, v12\n"
      "v_cmp_eq_u32 s[64:65], 1, v12\n"
      "v_cmp_eq_u32 s[66:67], 2, v12\n"
      "v_cmp_eq_u32 s[68:69], 3, v12\n"
      "v_lshrrev_b32 v136, 3, v1\n"
      "v_cndmask_b32 v6, v11, v10, s[62:63]\n"
      "v_cndmask_b32 v7, v11, v10, s[64:65]\n"
      "v_cndmask_b32 v8, v11, v10, s[66:67]\n"
      "v_cndmask_b32 v9, v11, v10, s[68:69]\n"
      "v_lshlrev_b32 v137, 7, v136\n"
      "v_lshl_or_b32 v137, v39, 3, v137\n"
      "s_lshl_b32 s27, s21, 10\n"
      "s_and_b32 s26, s27, 0x2000\n"
      "s_add_u32 s27, s27, s26\n"
      "s_add_u32 s27, s27, 24576\n"
      "v_add_u32 v138, s27, v137\n"
      "s_lshl_b32 s27, s21, 7\n"
      "v_lshl_add_u32 v139, v39, 3, v136\n"
      "v_lshl_add_u32 v139, v139, 1, s27\n"
      "s_mul_i32 s27, s21, 0x7000\n"
      "s_add_u32 s27, s27, 524288\n"
      "v_lshl_add_u32 v150, v1, 3, s27\n"
      "s_lshl_b32 s27, s20, 9\n"
      "v_add_u32 v151, s27, v150\n"
      "s_lshr_b32 s26, s3, 1\n"
      "s_mul_i32 s26, s26, 56\n"
      "s_add_u32 s26, s26, s22\n"
      "s_lshl_b32 s26, s26, 12\n"
      "s_lshl_b32 s27, s24, 9\n"
      "s_add_u32 s26, s26, s27\n"
      "s_add_u32 s26, s26, 65536\n"
      "v_lshl_add_u32 v141, v1, 3, s26\n"
      "v_lshlrev_b32 v148, 9, v136\n"
      "s_lshl_b32 s26, s20, 12\n"
      "s_lshl_b32 s27, s3, 7\n"
      "s_add_u32 s26, s26, s27\n"
      "s_add_u32 s26, s26, 65536\n"
      "v_add3_u32 v148, v148, v38, s26\n"
      "v_add_u32 v152, 0x38000, v148\n"
      "v_bfe_u32 v149, v0, 5, 1\n"
      "v_lshl_or_b32 v149, v39, 1, v149\n"
      "v_and_b32 v13, 7, v149\n"
      "v_lshrrev_b32 v149, 3, v149\n"
      "v_lshl_add_u32 v149, v13, 3, v149\n"
      "s_lshl_b32 s27, s3, 2\n"
      "v_lshl_add_u32 v149, v149, 1, s27\n"
      "v_and_b32 v13, 16, v0\n"
      "v_lshl_add_u32 v149, v13, 3, v149\n"
      "v_and_b32 v140, 1, v0\n"
      "v_cmp_eq_u32 s[62:63], 1, v140\n"
      "v_lshlrev_b32 v140, 6, v140\n"
      "v_and_or_b32 v140, v0, 48, v140\n"
      "v_mov_b32 v142, 0\n"
      "v_mov_b32 v143, 0x43380000\n"
      "v_mov_b32 v144, 0x43300000\n"
      "v_mov_b32 v145, 0xc0135761\n"
      "v_mov_b32 v146, 0\n"
      "s_mov_b32 s34, 0\n"
      "s_mov_b32 s36, 0\n"
      "s_mov_b32 s70, 0xfffff\n"
      "s_mov_b32 s71, 0xe0000000\n"
      "s_mov_b32 s72, 0x05040100\n"
      "s_mov_b32 s73, 0x07060302\n"
      "s_waitcnt vmcnt(7)\n"
      "buffer_load_dwordx4 v[72:75], v2, s[16:19], s48 offen nt\n"
      "ds_bpermute_b32 v10, v5, v4 offset:0\n"
      "ds_bpermute_b32 v11, v5, v4 offset:16\n"
      "s_waitcnt vmcnt(7)\n"
      "buffer_load_dwordx4 v[76:79], v2, s[16:19], s49 offen nt\n"
      "ds_bpermute_b32 v12, v5, v4 offset:32\n"
      "ds_bpermute_b32 v13, v5, v4 offset:48\n"
      "s_waitcnt vmcnt(7)\n"
      "buffer_load_dwordx4 v[80:83], v2, s[16:19], s50 offen nt\n"
      "ds_bpermute_b32 v14, v5, v4 offset:64\n"
      "ds_bpermute_b32 v15, v5, v4 offset:80\n"
      "s_waitcnt vmcnt(7)\n"
      "buffer_load_dwordx4 v[84:87], v2, s[16:19], s51 offen nt\n"
      "ds_bpermute_b32 v16, v5, v4 offset:96\n"
      "ds_bpermute_b32 v17, v5, v4 offset:112\n"
      "s_waitcnt vmcnt(7)\n"
      "buffer_load_dwordx4 v[88:91], v2, s[16:19], s52 offen nt\n"
      "ds_bpermute_b32 v18, v5, v4 offset:128\n"
      "ds_bpermute_b32 v19, v5, v4 offset:144\n"
      "s_waitcnt vmcnt(7)\n"
      "buffer_load_dwordx4 v[92:95], v2, s[16:19], s53 offen nt\n"
      "ds_bpermute_b32 v20, v5, v4 offset:160\n"
      "ds_bpermute_b32 v21, v5, v4 offset:176\n"
      "s_waitcnt vmcnt(7)\n"
      "buffer_load_dwordx4 v[96:99], v2, s[16:19], s54 offen nt\n"
      "ds_bpermute_b32 v22, v5, v4 offset:192\n"
      "ds_bpermute_b32 v23, v5, v4 offset:208\n"
      "s_waitcnt vmcnt(7)\n"
      "buffer_load_dwordx4 v[100:103], v2, s[16:19], s55 offen nt\n"
      "ds_bpermute_b32 v24, v5, v4 offset:224\n"
      "ds_bpermute_b32 v25, v5, v4 offset:240\n"
      "s_waitcnt lgkmcnt(0)\n"
      "s_add_u32 s60, s60, 1\n"
      "s_add_u32 s61, s61, 1\n"
      "v_mov_b32 v154, s60\n"
      "v_mov_b32 v155, s61\n"
      "v_cndmask_b32 v155, v154, v155, s[62:63]\n"
      "v_mov_b32 v157, v155\n"
      "v_perm_b32 v26, v10, v10, v6\n"
      "v_perm_b32 v27, v10, v10, v7\n"
      "v_perm_b32 v28, v10, v10, v8\n"
      "v_perm_b32 v29, v10, v10, v9\n"
      "s_nop 1\n"
      "v_mfma_f32_16x16x32_bf16 v[34:37], v[26:29], v[40:43], 0\n"
      "v_perm_b32 v30, v11, v11, v6\n"
      "v_perm_b32 v31, v11, v11, v7\n"
      "v_perm_b32 v32, v11, v11, v8\n"
      "v_perm_b32 v33, v11, v11, v9\n"
      "s_nop 1\n"
      "v_mfma_f32_16x16x32_bf16 v[34:37], v[30:33], v[44:47], v[34:37]\n"
      "v_perm_b32 v26, v12, v12, v6\n"
      "v_perm_b32 v27, v12, v12, v7\n"
      "v_perm_b32 v28, v12, v12, v8\n"
      "v_perm_b32 v29, v12, v12, v9\n"
      "s_nop 1\n"
      "v_mfma_f32_16x16x32_bf16 v[34:37], v[26:29], v[48:51], v[34:37]\n"
      "v_perm_b32 v30, v13, v13, v6\n"
      "v_perm_b32 v31, v13, v13, v7\n"
      "v_perm_b32 v32, v13, v13, v8\n"
      "v_perm_b32 v33, v13, v13, v9\n"
      "s_nop 1\n"
      "v_mfma_f32_16x16x32_bf16 v[34:37], v[30:33], v[52:55], v[34:37]\n"
      "v_perm_b32 v26, v14, v14, v6\n"
      "v_perm_b32 v27, v14, v14, v7\n"
      "v_perm_b32 v28, v14, v14, v8\n"
      "v_perm_b32 v29, v14, v14, v9\n"
      "s_nop 1\n"
      "v_mfma_f32_16x16x32_bf16 v[34:37], v[26:29], v[56:59], v[34:37]\n"
      "v_perm_b32 v30, v15, v15, v6\n"
      "v_perm_b32 v31, v15, v15, v7\n"
      "v_perm_b32 v32, v15, v15, v8\n"
      "v_perm_b32 v33, v15, v15, v9\n"
      "s_nop 1\n"
      "v_mfma_f32_16x16x32_bf16 v[34:37], v[30:33], v[60:63], v[34:37]\n"
      "v_perm_b32 v26, v16, v16, v6\n"
      "v_perm_b32 v27, v16, v16, v7\n"
      "v_perm_b32 v28, v16, v16, v8\n"
      "v_perm_b32 v29, v16, v16, v9\n"
      "s_nop 1\n"
      "v_mfma_f32_16x16x32_bf16 v[34:37], v[26:29], v[64:67], v[34:37]\n"
      "v_perm_b32 v30, v17, v17, v6\n"
      "v_perm_b32 v31, v17, v17, v7\n"
      "v_perm_b32 v32, v17, v17, v8\n"
      "v_perm_b32 v33, v17, v17, v9\n"
      "s_nop 1\n"
      "v_mfma_f32_16x16x32_bf16 v[34:37], v[30:33], v[68:71], v[34:37]\n"
      "v_perm_b32 v26, v18, v18, v6\n"
      "v_perm_b32 v27, v18, v18, v7\n"
      "v_perm_b32 v28, v18, v18, v8\n"
      "v_perm_b32 v29, v18, v18, v9\n"
      "s_waitcnt vmcnt(7)\n"
      "buffer_load_dwordx4 v[104:107], v3, s[28:31], 0 offen nt\n"
      "v_mfma_f32_16x16x32_bf16 v[34:37], v[26:29], v[72:75], v[34:37]\n"
      "v_perm_b32 v30, v19, v19, v6\n"
      "v_perm_b32 v31, v19, v19, v7\n"
      "v_perm_b32 v32, v19, v19, v8\n"
      "v_perm_b32 v33, v19, v19, v9\n"
      "s_waitcnt vmcnt(7)\n"
      "buffer_load_dwordx4 v[108:111], v3, s[28:31], 0 offen offset:2048 nt\n"
      "v_mfma_f32_16x16x32_bf16 v[34:37], v[30:33], v[76:79], v[34:37]\n"
      "v_perm_b32 v26, v20, v20, v6\n"
      "v_perm_b32 v27, v20, v20, v7\n"
      "v_perm_b32 v28, v20, v20, v8\n"
      "v_perm_b32 v29, v20, v20, v9\n"
      "s_waitcnt vmcnt(7)\n"
      "buffer_load_dwordx4 v[112:115], v3, s[28:31], s57 offen nt\n"
      "v_mfma_f32_16x16x32_bf16 v[34:37], v[26:29], v[80:83], v[34:37]\n"
      "v_perm_b32 v30, v21, v21, v6\n"
      "v_perm_b32 v31, v21, v21, v7\n"
      "v_perm_b32 v32, v21, v21, v8\n"
      "v_perm_b32 v33, v21, v21, v9\n"
      "s_waitcnt vmcnt(7)\n"
      "buffer_load_dwordx4 v[116:119], v3, s[28:31], s57 offen offset:2048 nt\n"
      "v_mfma_f32_16x16x32_bf16 v[34:37], v[30:33], v[84:87], v[34:37]\n"
      "v_perm_b32 v26, v22, v22, v6\n"
      "v_perm_b32 v27, v22, v22, v7\n"
      "v_perm_b32 v28, v22, v22, v8\n"
      "v_perm_b32 v29, v22, v22, v9\n"
      "s_waitcnt vmcnt(7)\n"
      "buffer_load_dwordx4 v[120:123], v3, s[28:31], s58 offen nt\n"
      "v_mfma_f32_16x16x32_bf16 v[34:37], v[26:29], v[88:91], v[34:37]\n"
      "v_perm_b32 v30, v23, v23, v6\n"
      "v_perm_b32 v31, v23, v23, v7\n"
      "v_perm_b32 v32, v23, v23, v8\n"
      "v_perm_b32 v33, v23, v23, v9\n"
      "s_waitcnt vmcnt(7)\n"
      "buffer_load_dwordx4 v[124:127], v3, s[28:31], s58 offen offset:2048 nt\n"
      "v_mfma_f32_16x16x32_bf16 v[34:37], v[30:33], v[92:95], v[34:37]\n"
      "v_perm_b32 v26, v24, v24, v6\n"
      "v_perm_b32 v27, v24, v24, v7\n"
      "v_perm_b32 v28, v24, v24, v8\n"
      "v_perm_b32 v29, v24, v24, v9\n"
      "s_waitcnt vmcnt(7)\n"
      "buffer_load_dwordx4 v[128:131], v3, s[28:31], s59 offen nt\n"
      "v_mfma_f32_16x16x32_bf16 v[34:37], v[26:29], v[96:99], v[34:37]\n"
      "v_perm_b32 v30, v25, v25, v6\n"
      "v_perm_b32 v31, v25, v25, v7\n"
      "v_perm_b32 v32, v25, v25, v8\n"
      "v_perm_b32 v33, v25, v25, v9\n"
      "s_waitcnt vmcnt(7)\n"
      "buffer_load_dwordx4 v[132:135], v3, s[28:31], s59 offen offset:2048 nt\n"
      "v_mfma_f32_16x16x32_bf16 v[34:37], v[30:33], v[100:103], v[34:37]\n"
      "s_nop 7\n"
      "v_add_f32_dpp v154, v35, v34 row_ror:8 row_mask:0xf bank_mask:0x3\n"
      "v_add_f32_dpp v154, v36, v37 row_ror:8 row_mask:0xf bank_mask:0xc\n"
      "global_store_dwordx2 v141, v[154:155], s[14:15] sc1\n"
      "s_waitcnt vmcnt(7)\n"
      "v_perm_b32 v40, v108, v104, s72\n"
      "v_perm_b32 v44, v108, v104, s73\n"
      "v_perm_b32 v48, v109, v105, s72\n"
      "v_perm_b32 v52, v109, v105, s73\n"
      "v_perm_b32 v56, v110, v106, s72\n"
      "v_perm_b32 v60, v110, v106, s73\n"
      "v_perm_b32 v64, v111, v107, s72\n"
      "v_perm_b32 v68, v111, v107, s73\n"
      "s_waitcnt vmcnt(5)\n"
      "global_load_dwordx4 v[28:31], v142, s[14:15] offset:256 sc1\n"
      "v_perm_b32 v41, v116, v112, s72\n"
      "v_perm_b32 v45, v116, v112, s73\n"
      "v_perm_b32 v49, v117, v113, s72\n"
      "v_perm_b32 v53, v117, v113, s73\n"
      "v_perm_b32 v57, v118, v114, s72\n"
      "v_perm_b32 v61, v118, v114, s73\n"
      "v_perm_b32 v65, v119, v115, s72\n"
      "v_perm_b32 v69, v119, v115, s73\n"
      "s_waitcnt vmcnt(4)\n"
      "v_perm_b32 v42, v124, v120, s72\n"
      "v_perm_b32 v46, v124, v120, s73\n"
      "v_perm_b32 v50, v125, v121, s72\n"
      "v_perm_b32 v54, v125, v121, s73\n"
      "v_perm_b32 v58, v126, v122, s72\n"
      "v_perm_b32 v62, v126, v122, s73\n"
      "v_perm_b32 v66, v127, v123, s72\n"
      "v_perm_b32 v70, v127, v123, s73\n"
      "s_waitcnt vmcnt(2)\n"
      "s_barrier\n"
      "global_load_dwordx4 v[160:163], v148, s[14:15] sc1\n"
      "global_load_dwordx4 v[164:167], v152, s[14:15] sc1\n"
      "v_perm_b32 v43, v132, v128, s72\n"
      "v_perm_b32 v47, v132, v128, s73\n"
      "v_perm_b32 v51, v133, v129, s72\n"
      "v_perm_b32 v55, v133, v129, s73\n"
      "v_perm_b32 v59, v134, v130, s72\n"
      "v_perm_b32 v63, v134, v130, s73\n"
      "v_perm_b32 v67, v135, v131, s72\n"
      "v_perm_b32 v71, v135, v131, s73\n"
      "s_waitcnt vmcnt(2)\n"
      "v_readfirstlane_b32 s76, v28\n"
      "v_readfirstlane_b32 s77, v29\n"
      "v_readfirstlane_b32 s79, v31\n"
      "s_cmp_eq_u32 s77, s60\n"
      "s_cbranch_scc0 L%=_flagwait\n"
      "s_cmp_eq_u32 s79, s61\n"
      "s_cbranch_scc0 L%=_flagwait\n"
      "L%=_flagok:\n"
      "s_add_u32 s35, s76, 0x3ff00000\n"
      "s_sub_u32 s37, 0x3ff00000, s76\n"
      "s_sub_u32 s26, 0xc3370000, s76\n"
      "v_mov_b32 v147, s26\n"
      "s_lshl_b32 s26, s76, 3\n"
      "s_sub_u32 s38, 0x56000000, s26\n"
      "s_sub_u32 s39, 0x4b800000, s26\n"
      "s_mov_b32 s56, 0\n"
      "L%=_poll:\n"
      "s_waitcnt vmcnt(0)\n"
      "v_permlane32_swap_b32 v160, v162\n"
      "v_permlane32_swap_b32 v164, v166\n"
      "v_add_f32 v28, v160, v162\n"
      "v_add_f32 v29, v164, v166\n"
      "v_cmp_eq_u32 vcc, s60, v161\n"
      "v_cmp_eq_u32 s[64:65], s61, v163\n"
      "v_permlane16_swap_b32 v28, v29\n"
      "v_add_f32 v28, v28, v29\n"
      "v_cmp_eq_u32 s[66:67], s60, v165\n"
      "v_cmp_eq_u32 s[68:69], s61, v167\n"
      "v_add_f32_dpp v28, v28, v28 row_ror:8 row_mask:0xf bank_mask:0xf\n"
      "v_cvt_pk_bf16_f32 v28, v28, v28\n"
      "v_and_b32 v28, 0xffff0000, v28\n"
      "v_mul_f32 v32, v28, v28\n"
      "v_fmamk_f32 v32, v32, 0xbdd2d3e7, v145\n"
      "v_mul_f32 v32, v32, v28\n"
      "v_exp_f32 v32, v32\n"
      "v_mov_b32 v29, v28\n"
      "v_add_f32 v32, 1.0, v32\n"
      "v_rcp_f32 v32, v32\n"
      "s_and_b64 vcc, vcc, s[64:65]\n"
      "v_mul_f32 v32, v32, v28\n"
      "v_permlane16_swap_b32 v28, v29\n"
      "v_cvt_pk_bf16_f32 v32, v32, v32\n"
      "v_and_b32 v32, 0xffff0000, v32\n"
      "v_mul_f32 v32, v32, v29\n"
      "v_cvt_pk_bf16_f32 v32, v32, v32\n"
      "ds_write_b16 v149, v32\n"
      "s_and_b64 s[66:67], s[66:67], s[68:69]\n"
      "s_and_b64 vcc, vcc, s[66:67]\n"
      "s_cmp_eq_u64 vcc, -1\n"
      "s_cbranch_scc0 L%=_again\n"
      "L%=_got:\n"
      "s_waitcnt lgkmcnt(0)\n"
      "s_barrier\n"
      "ds_read_b128 v[26:29], v140\n"
      "s_waitcnt lgkmcnt(0)\n"
      "v_mfma_f32_16x16x32_bf16 v[72:75], v[26:29], v[40:43], 0\n"
      "v_mfma_f32_16x16x32_bf16 v[76:79], v[26:29], v[44:47], 0\n"
      "v_mfma_f32_16x16x32_bf16 v[80:83], v[26:29], v[48:51], 0\n"
      "v_mfma_f32_16x16x32_bf16 v[84:87], v[26:29], v[52:55], 0\n"
      "v_mfma_f32_16x16x32_bf16 v[88:91], v[26:29], v[56:59], 0\n"
      "v_mfma_f32_16x16x32_bf16 v[92:95], v[26:29], v[60:63], 0\n"
      "v_mfma_f32_16x16x32_bf16 v[96:99], v[26:29], v[64:67], 0\n"
      "v_mfma_f32_16x16x32_bf16 v[100:103], v[26:29], v[68:71], 0\n"
      "s_nop 0\n"
      "v_add_f32_dpp v156, v73, v72 row_ror:8 row_mask:0x1 bank_mask:0x3\n"
      "v_add_f32_dpp v156, v76, v77 row_ror:8 row_mask:0x1 bank_mask:0xc\n"
      "v_add_f32_dpp v156, v81, v80 row_ror:8 row_mask:0x2 bank_mask:0x3\n"
      "v_add_f32_dpp v156, v84, v85 row_ror:8 row_mask:0x2 bank_mask:0xc\n"
      "v_add_f32_dpp v156, v89, v88 row_ror:8 row_mask:0x4 bank_mask:0x3\n"
      "v_add_f32_dpp v156, v92, v93 row_ror:8 row_mask:0x4 bank_mask:0xc\n"
      "v_add_f32_dpp v156, v97, v96 row_ror:8 row_mask:0x8 bank_mask:0x3\n"
      "v_add_f32_dpp v156, v100, v101 row_ror:8 row_mask:0x8 bank_mask:0xc\n"
      "v_cvt_f64_f32 v[30:31], v156\n"
      "v_cmp_nlt_f32 vcc, |v156|, s38\n"
      "v_fma_f64 v[30:31], v[30:31], s[34:35], v[142:143]\n"
      "v_add_u32 v31, 0xc0c82000, v31\n"
      "s_cbranch_vccnz L%=_unfit\n"
      "L%=_fit:\n"
      "global_atomic_add_x2 v[32:33], v138, v[30:31], s[14:15] sc0\n"
      "global_store_dwordx2 v151, v[156:157], s[14:15] sc1\n"
      "v_cmp_lt_f32 s[64:65], |v156|, s39\n"
      "s_waitcnt vmcnt(1)\n"
      "v_lshl_add_u64 v[30:31], v[30:31], 0, v[32:33]\n"
      "v_cmp_le_u32 vcc, s71, v31\n"
      "v_mov_b32 v33, v31\n"
      "v_and_or_b32 v31, v31, s70, v144\n"
      "v_fma_f64 v[30:31], v[30:31], s[36:37], v[146:147]\n"
      "s_and_b64 exec, exec, vcc\n"
      "s_cbranch_execz L%=_done\n"
      "v_cvt_f32_f64 v30, v[30:31]\n"
      "v_cvt_pk_bf16_f32 v31, v30, v30\n"
      "global_store_short v139, v31, s[12:13] sc1\n"
      "v_cmp_lt_f32 vcc, |v30|, s39\n"
      "s_and_b64 s[64:65], s[64:65], vcc\n"
      "v_cmp_le_u32 vcc, 0xe0100000, v33\n"
      "s_or_b64 s[64:65], s[64:65], vcc\n"
      "v_cmp_eq_u32 vcc, 0, v139\n"
      "s_and_b64 exec, exec, vcc\n"
      "s_cbranch_execz L%=_noepoch\n"
      "v_mov_b32 v30, s60\n"
      "v_mov_b32 v31, s61\n"
      "global_store_dwordx2 v139, v[30:31], s[14:15] sc1\n"
      "L%=_noepoch:\n"
      "s_cmp_lg_u64 s[64:65], 0\n"
      "s_cbranch_scc1 L%=_fallback\n"
      "L%=_done:\n"
      "s_endpgm\n"
      "L%=_again:\n"
      "global_load_dwordx4 v[160:163], v148, s[14:15] sc1\n"
      "global_load_dwordx4 v[164:167], v152, s[14:15] sc1\n"
      "s_add_u32 s56, s56, 1\n"
      "s_cmp_lt_u32 s56, 0x400000\n"
      "s_cbranch_scc1 L%=_poll\n"
      "s_branch L%=_got\n"
      "L%=_flagwait:\n"
      "s_mov_b32 s56, 0\n"
      "L%=_fw:\n"
      "global_load_dwordx4 v[28:31], v142, s[14:15] offset:256 sc1\n"
      "s_add_u32 s56, s56, 1\n"
      "s_waitcnt vmcnt(0)\n"
      "v_readfirstlane_b32 s76, v28\n"
      "v_readfirstlane_b32 s77, v29\n"
      "v_readfirstlane_b32 s79, v31\n"
      "s_cmp_eq_u32 s77, s60\n"
      "s_cselect_b32 s26, s79, s60\n"
      "s_cselect_b32 s27, s61, s77\n"
      "s_cmp_eq_u32 s26, s27\n"
      "s_cbranch_scc1 L%=_flagok\n"
      "s_cmp_lt_u32 s56, 0x400000\n"
      "s_cbranch_scc1 L%=_fw\n"
      "s_endpgm\n"
      "L%=_unfit:\n"
      "s_mov_b64 exec, vcc\n"
      "v_mov_b32 v30, 0\n"
      "v_mov_b32 v31, 0x04102000\n"
      "s_mov_b64 exec, -1\n"
      "s_branch L%=_fit\n"
      "L%=_fallback:\n"
      "s_mov_b64 exec, s[64:65]\n"
      "v_mov_b32 v30, 0\n"
      "s_mov_b32 s26, 0\n"
      "s_mov_b32 s56, 0\n"
      "L%=_fb:\n"
      "global_load_dwordx2 v[28:29], v150, s[14:15] sc1\n"
      "s_add_u32 s56, s56, 1\n"
      "s_waitcnt vmcnt(0)\n"
      "v_cmp_eq_u32 vcc, v29, v157\n"
      "s_cmp_eq_u64 vcc, exec\n"
      "s_cbranch_scc1 L%=_fbok\n"
      "s_cmp_lt_u32 s56, 0x400000\n"
      "s_cbranch_scc1 L%=_fb\n"
      "L%=_fbok:\n"
      "v_add_f32 v30, v30, v28\n"
      "v_add_u32 v150, 0x200, v150\n"
      "s_add_u32 s26, s26, 1\n"
      "s_cmp_lt_u32 s26, 56\n"
      "s_cbranch_scc1 L%=_fb\n"
      "v_cvt_pk_bf16_f32 v31, v30, v30\n"
      "global_store_short v139, v31, s[12:13] sc1\n"
      "s_branch L%=_done\n"
      "L%=_init:\n"
      "s_lshr_b32 s3, s3, 6\n"
      "v_lshrrev_b32 v2, 2, v0\n"
      "v_lshlrev_b32 v2, 7, v2\n"
      "v_and_b32 v3, 3, v0\n"
      "v_lshl_or_b32 v2, v3, 4, v2\n"
      "v_add_u32 v2, 24576, v2\n"
      "v_add_u32 v3, 0x4000, v2\n"
      "v_mov_b32 v4, 0\n"
      "v_mov_b32 v5, 0\n"
      "v_mov_b32 v6, 0\n"
      "v_mov_b32 v7, 0\n"
      "s_cmp_eq_u32 s3, 0\n"
      "s_movk_i32 s20, 0x400\n"
      "s_cselect_b32 s21, s20, 0x1a5c00\n"
      "v_lshlrev_b32 v8, 4, v1\n"
      "v_add_u32 v9, s21, v8\n"
      "s_waitcnt lgkmcnt(0)\n"
      "s_cselect_b64 s[16:17], s[4:5], s[6:7]\n"
      "s_cmp_eq_u32 s3, 2\n"
      "s_cselect_b64 s[16:17], s[8:9], s[16:17]\n"
      "s_cmp_eq_u32 s3, 3\n"
      "s_cselect_b64 s[16:17], s[10:11], s[16:17]\n"
      "global_store_dwordx4 v2, v[4:7], s[14:15] sc1\n"
      "global_store_dwordx4 v3, v[4:7], s[14:15] sc1\n"
      "global_load_dwordx4 v[12:15], v8, s[16:17] nt\n"
      "global_load_dwordx4 v[16:19], v9, s[16:17] nt\n"
      "s_load_dwordx2 s[60:61], s[14:15], 0x0\n"
      "s_mov_b32 s20, 0x7f807f80\n"
      "s_waitcnt vmcnt(0)\n"
      "v_and_b32 v12, s20, v12\n"
      "v_and_b32 v13, s20, v13\n"
      "v_and_b32 v14, s20, v14\n"
      "v_and_b32 v15, s20, v15\n"
      "v_and_b32 v16, s20, v16\n"
      "v_and_b32 v17, s20, v17\n"
      "v_and_b32 v18, s20, v18\n"
      "v_and_b32 v19, s20, v19\n"
      "v_pk_max_u16 v12, v12, v16\n"
      "v_pk_max_u16 v13, v13, v17\n"
      "v_pk_max_u16 v14, v14, v18\n"
      "v_pk_max_u16 v15, v15, v19\n"
      "v_pk_max_u16 v12, v12, v14\n"
      "v_pk_max_u16 v13, v13, v15\n"
      "v_pk_max_u16 v12, v12, v13\n"
      "v_lshrrev_b32 v13, 16, v12\n"
      "v_and_b32 v12, 0xffff, v12\n"
      "v_max_u32 v12, v12, v13\n"
      "v_lshrrev_b32 v12, 7, v12\n"
      "v_mov_b32 v13, 0\n"
      "s_lshl_b32 s22, s3, 2\n"
      "v_mov_b32 v14, s22\n"
      "ds_write_b32 v14, v13\n"
      "ds_max_u32 v14, v12\n"
      "s_waitcnt lgkmcnt(0)\n"
      "s_barrier\n"
      "s_cmp_lg_u32 s3, 0\n"
      "s_cbranch_scc1 L%=_end\n"
      "ds_read_b128 v[12:15], v13\n"
      "s_add_u32 s60, s60, 1\n"
      "s_add_u32 s61, s61, 1\n"
      "s_mov_b64 exec, 1\n"
      "v_mov_b32 v5, s60\n"
      "v_mov_b32 v7, s61\n"
      "v_mov_b32 v2, 256\n"
      "s_waitcnt lgkmcnt(0)\n"
      "v_add3_u32 v13, v13, v14, v15\n"
      "v_lshl_add_u32 v12, v12, 1, v13\n"
      "v_sub_u32 v12, 664, v12\n"
      "v_max_i32 v12, 0xffffffad, v12\n"
      "v_min_i32 v12, 0x96, v12\n"
      "v_lshlrev_b32 v4, 20, v12\n"
      "global_store_dwordx4 v2, v[4:7], s[14:15] sc1\n"
      "L%=_end:\n"
      :
      : "{s[0:1]}"(karg), "{s2}"(bid), "{v0}"(tid),
        "s"((unsigned)(unsigned long long)(__attribute__((address_space(3))) void*)hs)
      :
        "v1", "v2", "v3", "v4", "v5", "v6", "v7", "v8", "v9", "v10", "v11", "v12", "v13", "v14", "v15", "v16", "v17",
        "v18", "v19", "v20", "v21", "v22", "v23", "v24", "v25", "v26", "v27", "v28", "v29", "v30", "v31", "v32",
        "v33", "v34", "v35", "v36", "v37", "v38", "v39", "v40", "v41", "v42", "v43", "v44", "v45", "v46", "v47",
        "v48", "v49", "v50", "v51", "v52", "v53", "v54", "v55", "v56", "v57", "v58", "v59", "v60", "v61", "v62",
        "v63", "v64", "v65", "v66", "v67", "v68", "v69", "v70", "v71", "v72", "v73", "v74", "v75", "v76", "v77",
        "v78", "v79", "v80", "v81", "v82", "v83", "v84", "v85", "v86", "v87", "v88", "v89", "v90", "v91", "v92",
        "v93", "v94", "v95", "v96", "v97", "v98", "v99", "v100", "v101", "v102", "v103", "v104", "v105", "v106",
        "v107", "v108", "v109", "v110", "v111", "v112", "v113", "v114", "v115", "v116", "v117", "v118", "v119",
        "v120", "v121", "v122", "v123", "v124", "v125", "v126", "v127", "v128", "v129", "v130", "v131", "v132",
        "v133", "v134", "v135", "v136", "v137", "v138", "v139", "v140", "v141", "v142", "v143", "v144", "v145",
        "v146", "v147", "v148", "v149", "v150", "v151", "v152", "v153", "v154", "v155", "v156", "v157", "v158",
        "v159", "v160", "v161", "v162", "v163", "v164", "v165", "v166", "v167", "s3", "s4", "s5", "s6", "s7", "s8",
        "s9", "s10", "s11", "s12", "s13", "s14", "s15", "s16", "s17", "s18", "s19", "s20", "s21", "s22", "s23", "s24",
        "s25", "s26", "s27", "s28", "s29", "s30", "s31", "s34", "s35", "s36", "s37", "s38", "s39", "s41", "s42",
        "s43", "s44", "s45", "s46", "s47", "s48", "s49", "s50", "s51", "s52", "s53", "s54", "s55", "s56", "s57",
        "s58", "s59", "s60", "s61", "s62", "s63", "s64", "s65", "s66", "s67", "s68", "s69", "s70", "s71", "s72",
        "s73", "s74", "s75", "s76", "s77", "s78", "s79",
        "vcc", "scc", "memory");
#endif
}

extern "C" void kernel_launch(void* const* d_in, const int* in_sizes, int n_in, void* d_out, int out_size, void* d_ws,
                              size_t ws_size, hipStream_t stream) {
  geglu_fused<<<225, 256, 0, stream>>>(d_in[0], d_in[1], d_in[2], d_in[3], d_out, d_ws);
}
```
